```python
import torch

def _softmax(x, axis=-1):
    return torch.softmax(x, dim=axis)
def _gelu(x):
    return torch.nn.functional.gelu(x, approximate="tanh")
def _silu(x):
    return torch.nn.functional.silu(x)
def _sigmoid(x):
    return torch.sigmoid(x)
def _layernorm(x, axis=-1, eps=1e-5):
    m = x.mean(dim=axis, keepdim=True)
    v = x.var(dim=axis, keepdim=True, unbiased=False)
    return (x - m) / torch.sqrt(v + eps)
def _scatter_add(tgt, idx, src, axis=0):
    return tgt.clone().index_add_(axis, idx, src)
def _segment_sum(src, seg, n):
    out = torch.zeros((n,) + src.shape[1:], dtype=src.dtype)
    return out.index_add_(0, seg, src)
def _pad(x, pw):
    flat = tuple(v for pair in reversed(pw) for v in pair)
    return torch.nn.functional.pad(x, flat)

kernel_name = 'gated_mix__gqa_block_cached__glu_gated_reglu__decode'

def setup_inputs():

    _w = (lambda a: (lambda t: t.to(torch.bfloat16) if t.is_floating_point() else t)(torch.as_tensor(a)))
    def _A_setup():
        B, Sq, D = 1, 1, 1024
        Hq, Hkv, Sk, Dh = 32, 4, 65536, 128
        assert Hq % Hkv == 0, (Hq, Hkv)
        return {
            "x": _w((lambda *s: torch.randn(*s, dtype=torch.bfloat16) * 0.05)(B, Sq, D)),
            "k_cache": _w((lambda *s: torch.randn(*s, dtype=torch.bfloat16) * 3.2)(B, Hkv, Sk, Dh)),
            "v_cache": _w((lambda *s: torch.randn(*s, dtype=torch.bfloat16) * 1.6)(B, Hkv, Sk, Dh)),
            "wq": _w((lambda *s: torch.randn(*s, dtype=torch.bfloat16) * 3.2)(D, Hq * Dh) * (D ** -0.5)),
            "wo": _w((lambda *s: torch.randn(*s, dtype=torch.bfloat16) * 1.6)(Hq * Dh, D) * ((Hq * Dh) ** -0.5)),
        }
    _A_in = _A_setup()

    def _B_setup():
        B, S, D, F = 1, 1, 1024, 3584
        return {
            "x": _w((lambda *s: torch.randn(*s, dtype=torch.bfloat16) * 0.05)(B, S, D)),
            "W_gate": _w((lambda *s: torch.randn(*s, dtype=torch.bfloat16) * 1.6)(D, F) * (2.0 / D) ** 0.5),
            "W_up": _w((lambda *s: torch.randn(*s, dtype=torch.bfloat16) * 1.6)(D, F) * (2.0 / D) ** 0.5),
            "W_down": _w((lambda *s: torch.randn(*s, dtype=torch.bfloat16) * 1.6)(F, D) * (1.0 / F) ** 0.5),
        }
    _B_in = _B_setup()

    return {
        "A_x": _A_in["x"],
        "A_k_cache": _A_in["k_cache"],
        "A_v_cache": _A_in["v_cache"],
        "A_wq": _A_in["wq"],
        "A_wo": _A_in["wo"],
        "B_x": _B_in["x"],
        "B_W_gate": _B_in["W_gate"],
        "B_W_up": _B_in["W_up"],
        "B_W_down": _B_in["W_down"],
        "gate_w": _w((lambda *s: torch.randn(*s, dtype=torch.bfloat16) * 0.05)(1024) * 16.0),
    }

def reference(A_x, A_k_cache, A_v_cache, A_wq, A_wo, B_x, B_W_gate, B_W_up, B_W_down, gate_w):
    def _A_ref(x, k_cache, v_cache, wq, wo):
        B, Sq, D = x.shape
        Hq, Dh = 32, 128
        Hkv = k_cache.shape[1]
        rep = Hq // Hkv
        q = (lambda x, *d: x.permute(*d))((x @ wq).reshape(B, Sq, Hq, Dh), 0, 2, 1, 3)
        k = (lambda x, n, axis: torch.repeat_interleave(x, n, dim=axis))(k_cache, rep, axis=1)
        v = (lambda x, n, axis: torch.repeat_interleave(x, n, dim=axis))(v_cache, rep, axis=1)
        scores = torch.einsum("bhqd,bhkd->bhqk", q, k) * (Dh ** -0.5)
        p = _softmax(scores, axis=-1)
        o = torch.einsum("bhqk,bhkd->bhqd", p, v)
        return (lambda x, *d: x.permute(*d))(o, 0, 2, 1, 3).reshape(B, Sq, Hq * Dh) @ wo

    def _B_ref(x, W_gate, W_up, W_down):
        gate = torch.einsum("bsd,df->bsf", x, W_gate)
        up = torch.einsum("bsd,df->bsf", x, W_up)
        gate = gate * (lambda x, dt: x.to(dt))(gate > 0, gate.dtype)
        h = gate * up
        return torch.einsum("bsf,fd->bsd", h, W_down)

    x = A_x
    g = _sigmoid((x * gate_w).sum(-1, keepdims=True))
    a = _A_ref(x, A_k_cache, A_v_cache, A_wq, A_wo)
    b = _B_ref(x, B_W_gate, B_W_up, B_W_down)
    return g * a + (1 - g) * b
```

```cpp
#include <hip/hip_runtime.h>
#include <hip/hip_bf16.h>
#include <stdint.h>

namespace {

#define GAS __attribute__((address_space(1)))
#define LAS __attribute__((address_space(3)))
typedef unsigned v4u __attribute__((ext_vector_type(4)));
typedef unsigned v2u __attribute__((ext_vector_type(2)));
typedef float f32x4 __attribute__((ext_vector_type(4)));
typedef float f32x2 __attribute__((ext_vector_type(2)));
typedef __bf16 bf16x8 __attribute__((ext_vector_type(8)));
typedef __bf16 bf16x2 __attribute__((ext_vector_type(2)));

constexpr int DM = 1024, SK = 65536, FF = 3584;
constexpr int NWG = 256, NTHR = 512;
constexpr int WI = 12;
constexpr int WK = 7;
constexpr int WP = 5;
constexpr int TWSTEP = 29;
constexpr int RD_E = 3, RD_O = 1, GST = 4;

constexpr int AUX_NT = 2, AUX_SC1 = 16, RSRC_W3 = 0x00020000;
constexpr unsigned TAGM = 0x3FFu;
constexpr unsigned SPIN_CAP = 1u << 15;
constexpr unsigned OOB = 0x7ffffff0u;

constexpr unsigned WS_EPOCH = 0;
constexpr unsigned WS_CL = 1024;
constexpr unsigned WS_QP = 4096;
constexpr unsigned WS_GU = WS_QP + 4 * 4 * 1024 * 4;
constexpr unsigned WS_OP = WS_GU + 2 * 4 * FF * 4;
constexpr unsigned WS_ML = WS_OP + NWG * 4096;
constexpr unsigned WS_PO = WS_ML + NWG * 128;
constexpr unsigned WS_END = WS_PO + 32 * 128 * 128;

constexpr int L_RED = 0;
constexpr int L_Q = L_RED + 3 * 8 * 64 * 4;
constexpr int L_ML = L_Q + 2048;
constexpr int L_OC = L_ML + 512;
constexpr int L_OM = L_OC + 1024;
constexpr int L_FIN = L_OM + 64;
constexpr int L_XA = L_FIN + 1024;
constexpr int L_H = L_XA + 128;
constexpr int L_MISC = L_H + 128;
constexpr int L_SLAB = L_MISC + 48;
constexpr int L_END = L_SLAB + 4 * 8 * 132 * 4;

struct Params {
  const unsigned short *x, *k, *v, *wq, *wo, *wg, *wu, *wd, *gw;
  unsigned short* out;
  unsigned char* ws;
};

__device__ __forceinline__ __amdgpu_buffer_rsrc_t mkrs(const void* p, unsigned bytes) {
  return __builtin_amdgcn_make_buffer_rsrc(const_cast<void*>(p), 0, (int)bytes, RSRC_W3);
}
template <int AUX> __device__ __forceinline__ v4u bld(__amdgpu_buffer_rsrc_t rs, unsigned voff, unsigned soff) {
  return __builtin_amdgcn_raw_buffer_load_b128(rs, (int)voff, (int)soff, AUX);
}
__device__ __forceinline__ void bst_sc1(v4u v, __amdgpu_buffer_rsrc_t rs, unsigned voff) {
  __builtin_amdgcn_raw_buffer_store_b128(v, rs, (int)voff, 0, AUX_SC1);
}
__device__ __forceinline__ float u2f(unsigned u) { return __builtin_bit_cast(float, u); }
__device__ __forceinline__ unsigned f2u(float f) { return __builtin_bit_cast(unsigned, f); }
__device__ __forceinline__ float bflo(unsigned u) { return u2f(u << 16); }
__device__ __forceinline__ float bfhi(unsigned u) { return u2f(u & 0xffff0000u); }
__device__ __forceinline__ unsigned tagv(float v, unsigned tag) { return ((f2u(v) + 0x200u) & ~TAGM) | tag; }
__device__ __forceinline__ float untag(unsigned u) { return u2f(u & ~TAGM); }
__device__ __forceinline__ unsigned bad4(v4u a, unsigned tag) { return ((a.x ^ tag) | (a.y ^ tag)) | ((a.z ^ tag) | (a.w ^ tag)); }
template <int CTRL> __device__ __forceinline__ float dpp(float x) {
  return u2f((unsigned)__builtin_amdgcn_mov_dpp((int)f2u(x), CTRL, 0xf, 0xf, true));
}
constexpr int ROR1 = 0x121, ROR2 = 0x122, ROR4 = 0x124, ROR8 = 0x128;
__device__ __forceinline__ float sw32_add(float a, float b) {
  auto s = __builtin_amdgcn_permlane32_swap(f2u(a), f2u(b), false, false);
  return u2f(s[0]) + u2f(s[1]);
}
__device__ __forceinline__ float sw16_add(float a, float b) {
  auto s = __builtin_amdgcn_permlane16_swap(f2u(a), f2u(b), false, false);
  return u2f(s[0]) + u2f(s[1]);
}
__device__ __forceinline__ float xrow_max(float x) {
  auto s = __builtin_amdgcn_permlane16_swap(f2u(x), f2u(x), false, false);
  x = fmaxf(u2f(s[0]), u2f(s[1]));
  auto t = __builtin_amdgcn_permlane32_swap(f2u(x), f2u(x), false, false);
  return fmaxf(u2f(t[0]), u2f(t[1]));
}
__device__ __forceinline__ float xrow_sum(float x) { x = sw16_add(x, x); return sw32_add(x, x); }
__device__ __forceinline__ float row_sum(float x) { x += dpp<ROR8>(x); x += dpp<ROR4>(x); x += dpp<ROR2>(x); x += dpp<ROR1>(x); return x; }
__device__ __forceinline__ float hi3_sum(float x) { x += dpp<ROR8>(x); return xrow_sum(x); }
__device__ __forceinline__ float hi3_max(float x) { x = fmaxf(x, dpp<ROR8>(x)); return xrow_max(x); }
__device__ __forceinline__ float red4(f32x4 v) {
  const float a = sw16_add(sw32_add(v[0], v[2]), sw32_add(v[1], v[3]));
  return a + dpp<ROR8>(a);
}
__device__ __forceinline__ f32x4 mfma32(v4u a, v4u b, f32x4 c) {
  return __builtin_amdgcn_mfma_f32_16x16x32_bf16(__builtin_bit_cast(bf16x8, a), __builtin_bit_cast(bf16x8, b), c, 0, 0, 0);
}
__device__ __forceinline__ unsigned pkbf(float lo, float hi) {
  bf16x2 t = {(__bf16)lo, (__bf16)hi};
  return __builtin_bit_cast(unsigned, t);
}
__device__ __forceinline__ void gemv4(const v4u (&w)[4], const float (&xs)[4], f32x2 (&a)[4]) {
#pragma unroll
  for (int t = 0; t < 4; ++t)
#pragma unroll
    for (int k = 0; k < 4; ++k) a[k] += xs[t] * f32x2{bflo(w[t][k]), bfhi(w[t][k])};
}
__device__ __forceinline__ float red8(const f32x2 (&a2)[4], int lane) {
  const float a[8] = {a2[0].x, a2[0].y, a2[1].x, a2[1].y, a2[2].x, a2[2].y, a2[3].x, a2[3].y};
  float b[4], c[2];
#pragma unroll
  for (int i = 0; i < 4; ++i) b[i] = sw32_add(a[i], a[i + 4]);
#pragma unroll
  for (int i = 0; i < 2; ++i) c[i] = sw16_add(b[i], b[i + 2]);
  const float c0 = c[0] + dpp<ROR8>(c[0]), c1 = c[1] + dpp<ROR8>(c[1]);
  return (lane & 8) ? c1 : c0;
}
#define SB() __builtin_amdgcn_sched_barrier(0)
#define BAR() do { asm volatile("s_waitcnt lgkmcnt(0)" ::: "memory"); __builtin_amdgcn_s_barrier(); asm volatile("" ::: "memory"); } while (0)
#define FENCE() asm volatile("" ::: "memory")

__global__ void __launch_bounds__(NTHR, 2) __attribute__((amdgpu_waves_per_eu(2, 2))) fused_decode(Params p) {
  __shared__ __attribute__((aligned(16))) unsigned char smem[L_END];
  LAS unsigned char* L = (LAS unsigned char*)smem;
  const int tid = threadIdx.x, lane = tid & 63, w = __builtin_amdgcn_readfirstlane(tid >> 6);
  const int b = blockIdx.x;
  const int G = (b >> 3) & 3, j = (b >> 5) * 8 + (b & 7);
  const int kq = j >> 4, c16 = j & 15;
  const int g = lane >> 4, c = lane & 15;
  __builtin_amdgcn_s_setprio(3);
  const __amdgpu_buffer_rsrc_t rws = mkrs(p.ws, WS_END);
  const unsigned epoch_raw = __builtin_amdgcn_raw_buffer_load_b32(rws, WS_EPOCH, 0, AUX_SC1);
  const int hw = w & 3;
  const bool has_gu = c16 < 14;
  const int ksl = j >> 1, ns = j & 1, ks = G * 32 + ksl;
  LAS float* red = (LAS float*)(L + L_RED);
  volatile LAS unsigned* misc = (volatile LAS unsigned*)(L + L_MISC);
  const int rcol = 8 * (lane & 7) + 4 * (lane >> 5) + 2 * ((lane >> 4) & 1) + ((lane >> 3) & 1);
  const f32x4 z4 = {0.f, 0.f, 0.f, 0.f};
  const __amdgpu_buffer_rsrc_t ro = mkrs(p.wo + (size_t)(1024 * G + 32 * ksl) * DM + 512 * ns + 64 * w, 32 * 2048 - 1024 * ns - 128 * w);
  const __amdgpu_buffer_rsrc_t rd = mkrs(p.wd + (size_t)(28 * ks) * DM + 512 * ns + 64 * w, 28 * 2048 - 1024 * ns - 128 * w);
  const unsigned vow = (lane >> 3) * 4 * 2048 + (lane & 7) * 16;
  v4u TW[8];
  f32x2 a[4], wo_f[4][4], wd_f[4][4];
  float gs;
  auto twprep_w = [&]() {
    asm volatile("" : "+v"(TW[0]), "+v"(TW[1]), "+v"(TW[2]), "+v"(TW[3]), "+v"(TW[4]), "+v"(TW[5]), "+v"(TW[6]), "+v"(TW[7]));
#pragma unroll
    for (int t = 0; t < 4; ++t)
#pragma unroll
      for (int k = 0; k < 4; ++k) { wo_f[t][k] = f32x2{bflo(TW[t][k]), bfhi(TW[t][k])}; wd_f[t][k] = f32x2{bflo(TW[4 + t][k]), bfhi(TW[4 + t][k])}; }
#pragma unroll
    for (int t = 0; t < 4; ++t) {
      asm volatile("" : "+v"(wo_f[t][0]), "+v"(wo_f[t][1]), "+v"(wo_f[t][2]), "+v"(wo_f[t][3]));
      asm volatile("" : "+v"(wd_f[t][0]), "+v"(wd_f[t][1]), "+v"(wd_f[t][2]), "+v"(wd_f[t][3]));
    }
  };
  auto twprep_d = [&]() {
    const f32x4 hv = *(LAS const f32x4*)(L + L_H + 16 * (lane >> 3));
    gs = u2f(misc[0]);
    const float gn = 1.f - gs;
#pragma unroll
    for (int i = 0; i < 4; ++i) a[i] = (hv[0] * gn) * wd_f[0][i];
#pragma unroll
    for (int t = 1; t < 4; ++t)
#pragma unroll
      for (int i = 0; i < 4; ++i) a[i] += (hv[t] * gn) * wd_f[t][i];
    asm volatile("" : "+v"(a[0]), "+v"(a[1]), "+v"(a[2]), "+v"(a[3]));
  };
  unsigned tag, epoch_v;
  if (w == 0) {
    if (lane < 8) { LAS unsigned* mlp = (LAS unsigned*)(L + L_ML) + lane;
#pragma unroll
      for (int ww = 0; ww < 4; ++ww) mlp[ww * 8] = 0xffffffffu; }
    if (lane < 9) ((LAS unsigned*)(L + L_MISC))[1 + lane] = 0u;
  }
  if (w >= 4) {
    const int rq0 = 256 * kq + 64 * hw;
    const v2u xq0 = *(const GAS v2u*)(p.x + rq0 + 4 * (lane >> 3)), xq1 = *(const GAS v2u*)(p.x + rq0 + 32 + 4 * (lane >> 3));
    v4u wa[8];
    {
      const __amdgpu_buffer_rsrc_t rq = mkrs(p.wq + (size_t)rq0 * 4096 + (16 * G + c16) * 64, 64 * 8192);
      const unsigned vo = (lane >> 3) * 4 * 8192 + (lane & 7) * 16;
#pragma unroll
      for (int t = 0; t < 8; ++t) wa[t] = bld<AUX_NT>(rq, vo, (t & 3) * 8192 + (t >> 2) * 32 * 8192);
    }
    SB();
    BAR();
    epoch_v = __builtin_amdgcn_readfirstlane(epoch_raw);
    const unsigned tag32 = epoch_v + 1u;
    tag = tag32 & TAGM;
    const bool xeven = (__builtin_amdgcn_s_getreg(20 | (3 << 11)) & 1) == 0;
    int tcA, tcB, tcC;
    bool upA = false, upC = true;
    if (has_gu) {
      tcA = c16; tcB = c16; tcC = xeven ? (c16 ^ 1) : -1;
      if ((c16 >> 1) < RD_E + RD_O) { upC = false; if ((c16 >> 1) >= GST) tcC = -1; }
    }
    else if (xeven) { upA = true; const int o_ = 1 - (c16 & 1); tcA = RD_E > 0 ? o_ : -1; tcB = RD_E > 1 ? 2 + o_ : -1; tcC = RD_E > 2 ? 4 + o_ : -1; }
    else { upA = true; const int o_ = 2 * RD_E + (c16 & 1); tcA = RD_O > 0 ? o_ : -1; tcB = RD_O > 1 ? 2 + o_ : -1; tcC = -1; }
    if (hw == 0 && lane < 3) {
      const int tc = lane == 0 ? tcA : lane == 1 ? tcB : tcC;
      if (tc >= 0 && tc != c16 && (lane == 2 || !has_gu))
        __hip_atomic_store((GAS unsigned*)(p.ws + WS_CL + ((lane == 2 && !upC) ? 1024 : 0) + ((G * 4 + kq) * 16 + tc) * 4), tag32, __ATOMIC_RELAXED, __HIP_MEMORY_SCOPE_AGENT);
    }
    const float xa[4] = {bflo(xq0.x), bfhi(xq0.x), bflo(xq0.y), bfhi(xq0.y)}, xb[4] = {bflo(xq1.x), bfhi(xq1.x), bflo(xq1.y), bfhi(xq1.y)};
    auto tile = [&](const v4u (&wt)[8], int mat, int cnt, unsigned dst) {
      f32x2 a[4];
#pragma unroll
      for (int i = 0; i < 4; ++i) a[i] = f32x2{0.f, 0.f};
      const v4u w0[4] = {wt[0], wt[1], wt[2], wt[3]}, w1[4] = {wt[4], wt[5], wt[6], wt[7]};
      gemv4(w0, xa, a);
      gemv4(w1, xb, a);
      red[(mat * 4 + hw) * 64 + rcol] = red8(a, lane);
      asm volatile("s_waitcnt lgkmcnt(0)" ::: "memory");
      unsigned old = 0;
      if (lane == 0) old = __hip_atomic_fetch_add((LAS unsigned*)(L + L_MISC) + cnt, 1u, __ATOMIC_RELAXED, __HIP_MEMORY_SCOPE_WORKGROUP);
      old = __builtin_amdgcn_readfirstlane(old);
      if (old == 3 && lane < 16) {
        FENCE();
        LAS const f32x4* r4 = (LAS const f32x4*)(L + L_RED) + mat * 64;
        const f32x4 sv = (r4[lane] + r4[16 + lane]) + (r4[32 + lane] + r4[48 + lane]);
        const v4u o = {tagv(sv[0], tag), tagv(sv[1], tag), tagv(sv[2], tag), tagv(sv[3], tag)};
        bst_sc1(o, rws, dst + lane * 16);
      }
    };
    tile(wa, 0, 2, WS_QP + ((G * 4 + kq) * 1024 + c16 * 64) * 4);
    __builtin_amdgcn_s_setprio(2);
    {
      v4u wb[8], wc[8], we[8];
      unsigned flu = 0;
      const unsigned vo = (lane >> 3) * 4 * 7168 + (lane & 7) * 16;
      if (!xeven && has_gu) {
        const unsigned fu = __builtin_amdgcn_raw_buffer_load_b32(rws, WS_CL + ((G * 4 + kq) * 16 + c16) * 4, 0, AUX_SC1);
        if (GST > 0 && (c16 >> 1) < GST) {
          const unsigned fg = __builtin_amdgcn_raw_buffer_load_b32(rws, WS_CL + 1024 + ((G * 4 + kq) * 16 + c16) * 4, 0, AUX_SC1);
          if (__builtin_amdgcn_readfirstlane(fg) == tag32) tcA = -1;
        }
        flu = fu;
      }
      const int cA = G * 14 + (tcA < 0 ? 0 : tcA), cC = G * 14 + (tcC < 0 ? 0 : tcC);
      const __amdgpu_buffer_rsrc_t rg = mkrs((upA ? p.wu : p.wg) + (size_t)rq0 * FF + cA * 64, tcA >= 0 ? 64 * 7168 - cA * 128 : 0);
#pragma unroll
      for (int t = 0; t < 8; ++t) wb[t] = bld<AUX_NT>(rg, vo + (t & 3) * 7168, (t >> 2) * 32 * 7168);
      SB();
      if (!xeven && has_gu && __builtin_amdgcn_readfirstlane(flu) == tag32) tcB = -1;
      const int cB = G * 14 + (tcB < 0 ? 0 : tcB);
      const __amdgpu_buffer_rsrc_t ru = mkrs(p.wu + (size_t)rq0 * FF + cB * 64, tcB >= 0 ? 64 * 7168 - cB * 128 : 0),
                                   rn = mkrs((upC ? p.wu : p.wg) + (size_t)rq0 * FF + cC * 64, tcC >= 0 ? 64 * 7168 - cC * 128 : 0);
#pragma unroll
      for (int t = 0; t < 8; ++t) wc[t] = bld<AUX_NT>(ru, vo + (t & 3) * 7168, (t >> 2) * 32 * 7168);
#pragma unroll
      for (int t = 0; t < 8; ++t) we[t] = bld<AUX_NT>(rn, vo + (t & 3) * 7168, (t >> 2) * 32 * 7168);
      SB();
      if (tcA >= 0) tile(wb, 1, 3, WS_GU + (((upA ? 1 : 0) * 4 + kq) * FF + cA * 64) * 4);
      if (tcB >= 0) tile(wc, 2, 8, WS_GU + ((1 * 4 + kq) * FF + cB * 64) * 4);
      if (tcC >= 0) tile(we, 3, 9, WS_GU + (((upC ? 1 : 0) * 4 + kq) * FF + cC * 64) * 4);
    }
    __builtin_amdgcn_s_setprio(3);
    unsigned ok = 0;
    v4u qv[4];
    for (unsigned spin = 0; spin < SPIN_CAP; ++spin) {
      FENCE();
#pragma unroll
      for (int n = 0; n < 4; ++n) qv[n] = bld<AUX_SC1>(rws, lane * 16 + hw * 1024, WS_QP + (G * 4 + n) * 4096);
      if (__all(((bad4(qv[0], tag) | bad4(qv[1], tag) | bad4(qv[2], tag) | bad4(qv[3], tag)) & TAGM) == 0)) { ok = 1; break; }
      __builtin_amdgcn_s_sleep(2);
    }
    float sq[4];
#pragma unroll
    for (int e = 0; e < 4; ++e) sq[e] = (untag(qv[0][e]) + untag(qv[1][e])) + (untag(qv[2][e]) + untag(qv[3][e]));
    const v2u o = {pkbf(sq[0], sq[1]), pkbf(sq[2], sq[3])};
    *(LAS v2u*)(L + L_Q + (256 * hw + 4 * lane) * 2) = o;
    if (lane == 0 && !ok) misc[1] = 1u;
    BAR();
    __builtin_amdgcn_s_setprio(1);
    if (hw == 0) {
      const int q7 = (lane & 7) < 7 ? (lane & 7) : 6;
      const unsigned off = WS_GU + (((lane >> 5) * 4 + ((lane >> 3) & 3)) * FF + 28 * ks + 4 * q7) * 4;
      v4u gv;
      unsigned okh = 0;
      for (unsigned spin = 0; spin < SPIN_CAP; ++spin) {
        FENCE();
        gv = bld<AUX_SC1>(rws, off, 0);
        if (__all((bad4(gv, tag) & TAGM) == 0)) { okh = 1; break; }
        __builtin_amdgcn_s_sleep(8);
      }
      f32x4 hv;
#pragma unroll
      for (int e = 0; e < 4; ++e) {
        float t = untag(gv[e]);
        t += dpp<ROR8>(t); t = sw16_add(t, t);
        auto sx = __builtin_amdgcn_permlane32_swap(f2u(t), f2u(t), false, false);
        const float gg = u2f(sx[0]), uu = u2f(sx[1]);
        hv[e] = (lane & 7) < 7 ? (gg > 0.f ? gg : 0.f * gg) * uu : 0.f;
      }
      if (lane < 8) *(LAS f32x4*)(L + L_H + 16 * lane) = hv;
      if (lane == 0 && !okh) misc[1] = 1u;
    } else if (hw == 1) {
      const v4u x0 = *(const GAS v4u*)(p.x + 16 * lane), x1 = *(const GAS v4u*)(p.x + 16 * lane + 8);
      const v4u g0 = *(const GAS v4u*)(p.gw + 16 * lane), g1 = *(const GAS v4u*)(p.gw + 16 * lane + 8);
      float sg = 0.f;
#pragma unroll
      for (int k = 0; k < 4; ++k) {
        sg = fmaf(bflo(x0[k]), bflo(g0[k]), sg); sg = fmaf(bfhi(x0[k]), bfhi(g0[k]), sg);
        sg = fmaf(bflo(x1[k]), bflo(g1[k]), sg); sg = fmaf(bfhi(x1[k]), bfhi(g1[k]), sg);
      }
      sg = xrow_sum(row_sum(sg));
      if (lane == 0) misc[0] = f2u(1.f / (1.f + __expf(-sg)));
    }
    while (misc[4 + hw] == 0u) __builtin_amdgcn_s_sleep(4);
    __builtin_amdgcn_s_setprio(2);
#pragma unroll
    for (int q_ = 0; q_ < 4; ++q_) TW[q_] = bld<AUX_NT>(ro, vow + q_ * 2048, 0);
#pragma unroll
    for (int q_ = 0; q_ < 4; ++q_) TW[4 + q_] = bld<AUX_NT>(rd, vow + q_ * 2048, 0);
    SB();
    BAR();
  } else {
    const size_t hb = (size_t)G * SK * 128 + (size_t)(8 * j + 2 * w) * 2048;
    const unsigned hn = (SK * 256) - (8 * j + 2 * w) * 4096;
    const __amdgpu_buffer_rsrc_t rk = mkrs(p.k + hb, hn), rv = mkrs(p.v + hb, hn);
    const unsigned vok = (lane & 7) * 256 + (((lane >> 3) & 1) * 4 + g) * 16;
    const unsigned vov = g * 1024 + c * 16;
    unsigned proff[8];
#pragma unroll
    for (int pi = 0; pi < 8; ++pi) proff[pi] = ((pi + G) & 7) * (512u * 4096u);
    constexpr int NU = 34;
    v4u U[NU][4];
#define ISSUE(n) do { _Pragma("unroll") for (int q_ = 0; q_ < 4; ++q_) { \
    if ((n) < 16) U[n][q_] = bld<AUX_NT>(rk, vok + (q_ >> 1) * 2048 + (q_ & 1) * 128, proff[(n) >> 1] + ((n) & 1) * 4096u); \
    else if ((n) < 32) U[n][q_] = bld<AUX_NT>(rv, vov + q_ * 256, proff[((n) - 16) >> 1] + ((n) & 1) * 4096u); \
    else if ((n) == 32) U[n][q_] = bld<AUX_NT>(ro, vow + q_ * 2048, 0); \
    else U[n][q_] = bld<AUX_NT>(rd, vow + q_ * 2048, 0); } SB(); } while (0)
    __builtin_amdgcn_s_setprio(1);
    ISSUE(0);
    BAR();
#pragma unroll
    for (int n = 1; n < WI; ++n) ISSUE(n);
    epoch_v = __builtin_amdgcn_readfirstlane(epoch_raw);
    tag = (epoch_v + 1u) & TAGM;
    BAR();
    v4u QA[2], QB[2];
    {
      const int hi = (lane >> 3) & 1;
#pragma unroll
      for (int u = 0; u < 2; ++u) {
        QA[u] = *(LAS const v4u*)(L + L_Q + (lane & 7) * 256 + (8 * u + 4 * hi + g) * 16);
        QB[u] = *(LAS const v4u*)(L + L_Q + (lane & 7) * 256 + (8 * u + 4 * (1 - hi) + g) * 16);
      }
    }
    const bool validA = (lane >> 5) == ((lane >> 3) & 1);
    float S[8][4];
    f32x4 P1, P2;
#pragma unroll
    for (int n = 0; n < 16; ++n) {
      if (n + WK >= WI && n + WK < 16 + WP) ISSUE(n + WK);
      if ((n & 1) == 0) {
        P1 = mfma32(U[n][0], QA[0], z4); P1 = mfma32(U[n][1], QA[1], P1);
        P2 = mfma32(U[n][2], QA[0], z4); P2 = mfma32(U[n][3], QA[1], P2);
      } else {
        f32x4 R1 = mfma32(U[n][0], QB[0], z4); R1 = mfma32(U[n][1], QB[1], R1);
        f32x4 R2 = mfma32(U[n][2], QB[0], z4); R2 = mfma32(U[n][3], QB[1], R2);
#pragma unroll
        for (int i = 0; i < 4; ++i) {
          const float m1 = validA ? P1[i] : R1[i], m2 = validA ? P2[i] : R2[i];
          auto sx = __builtin_amdgcn_permlane32_swap(f2u(m1), f2u(m2), false, false);
          S[n >> 1][i] = u2f(sx[0]) + dpp<ROR8>(u2f(sx[1]));
        }
      }
      SB();
    }
    constexpr float C2 = 0.08838834764831845f * 1.4426950408889634f;
    float mx = S[0][0];
#pragma unroll
    for (int qd = 0; qd < 8; ++qd)
#pragma unroll
      for (int i = 0; i < 4; ++i) mx = fmaxf(mx, S[qd][i]);
    mx = hi3_max(mx) * C2;
    if (lane < 8) ((LAS float*)(L + L_ML))[w * 8 + lane] = mx;
    float ls = 0.f;
    f32x4 acc[8];
#pragma unroll
    for (int i = 0; i < 8; ++i) acc[i] = z4;
    float Mcu = 0.f, omw = 0.f;
#pragma unroll
    for (int n = 16; n < 32; ++n) {
      if (n + WP < NU) ISSUE(n + WP);
      if (n == TWSTEP && lane == 0) misc[4 + w] = 1u;
      if ((n & 1) == 0) {
        const int qd = (n - 16) >> 1;
#pragma unroll
        for (int i = 0; i < 4; ++i) { S[qd][i] = __builtin_amdgcn_exp2f(fmaf(S[qd][i], C2, -mx)); ls += S[qd][i]; }
        if (n == 30) ls = hi3_sum(ls);
        SB();
      }
      if (n == 30) {
        LAS const volatile unsigned* mlp = (LAS const volatile unsigned*)(L + L_ML) + (lane & 7);
        unsigned mm[4];
        for (;;) {
          bool all = true;
#pragma unroll
          for (int ww = 0; ww < 4; ++ww) { mm[ww] = mlp[ww * 8]; all = all && mm[ww] != 0xffffffffu; }
          if (__all(all)) break;
          __builtin_amdgcn_s_sleep(1);
        }
        Mcu = fmaxf(fmaxf(u2f(mm[0]), u2f(mm[1])), fmaxf(u2f(mm[2]), u2f(mm[3])));
        omw = __builtin_amdgcn_exp2f(mx - Mcu);
        if (lane < 8) { ((LAS float*)(L + L_ML))[32 + w * 8 + lane] = ls * omw; if (w == 0) ((LAS float*)(L + L_ML))[64 + lane] = Mcu; }
        SB();
      }
      if (n & 1) {
        const int qd = (n - 16) >> 1;
        const unsigned a0 = pkbf(S[qd][0], S[qd][1]), a1 = pkbf(S[qd][2], S[qd][3]);
        const v4u PPq = {a0, a1, f2u(dpp<ROR8>(u2f(a0))), f2u(dpp<ROR8>(u2f(a1)))};
#pragma unroll
        for (int wd = 0; wd < 4; ++wd)
#pragma unroll
          for (int hf = 0; hf < 2; ++hf) {
            const unsigned sel = hf ? 0x07060302u : 0x05040100u;
            const v4u A = {__builtin_amdgcn_perm(U[n - 1][1][wd], U[n - 1][0][wd], sel), __builtin_amdgcn_perm(U[n - 1][3][wd], U[n - 1][2][wd], sel),
                           __builtin_amdgcn_perm(U[n][1][wd], U[n][0][wd], sel), __builtin_amdgcn_perm(U[n][3][wd], U[n][2][wd], sel)};
            acc[2 * wd + hf] = mfma32(A, PPq, acc[2 * wd + hf]);
          }
      }
      SB();
    }
#pragma unroll
    for (int n = 32 + WP; n < NU; ++n) ISSUE(n);
#undef ISSUE
    if (c < 8) {
      LAS f32x4* sl = (LAS f32x4*)(L + L_SLAB + ((w * 8 + c) * 132 + 32 * g) * 4);
#pragma unroll
      for (int rr = 0; rr < 4; ++rr) {
        sl[2 * rr] = f32x4{acc[0][rr], acc[1][rr], acc[2][rr], acc[3][rr]} * omw;
        sl[2 * rr + 1] = f32x4{acc[4][rr], acc[5][rr], acc[6][rr], acc[7][rr]} * omw;
      }
    }
#pragma unroll
    for (int q_ = 0; q_ < 4; ++q_) { TW[q_] = U[32][q_]; TW[4 + q_] = U[33][q_]; }
    BAR();
  }
  if (w >= 4) {
    const int t2 = tid - 256, h = t2 >> 5, d4 = t2 & 31;
    f32x4 sv[4];
#pragma unroll
    for (int ww = 0; ww < 4; ++ww) sv[ww] = *(LAS const f32x4*)(L + L_SLAB + ((ww * 8 + h) * 132 + 4 * d4) * 4);
    LAS const float* mlp = (LAS const float*)(L + L_ML);
    const float l0 = mlp[32 + h], l1 = mlp[40 + h], l2 = mlp[48 + h], l3 = mlp[56 + h], Mh = mlp[64 + h];
    SB();
    const f32x4 o = (sv[0] + sv[1]) + (sv[2] + sv[3]);
    const v4u ov = {tagv(o[0], tag), tagv(o[1], tag), tagv(o[2], tag), tagv(o[3], tag)};
    bst_sc1(ov, rws, WS_OP + ((G * 32 + h * 4 + (d4 >> 3)) * 64 + j) * 128 + (d4 & 7) * 16);
    {
      const unsigned mb = f2u(Mh), lb = f2u((l0 + l1) + (l2 + l3));
      const v4u mv = {(mb & 0xffff0000u) | tag, (mb << 16) | tag, (lb & 0xffff0000u) | tag, (lb << 16) | tag};
      bst_sc1(mv, rws, d4 == 0 ? WS_ML + ((G * 8 + h) * 64 + j) * 16 : OOB);
    }
    SB();
    twprep_w();
    twprep_d();
  } else {
    twprep_w();
    twprep_d();
  }
  const int hh = ksl >> 2;
  const unsigned off_o = WS_OP + (G * 32 + ksl) * 8192 + w * 1024 + lane * 16;
  const unsigned off_m = WS_ML + ((G * 8 + hh) * 64 + 8 * w + (lane >> 3)) * 16;
  v4u ov, mv;
  __builtin_amdgcn_s_sleep(10);
  FENCE();
  ov = bld<AUX_SC1>(rws, off_o, 0); mv = bld<AUX_SC1>(rws, off_m, 0);
  SB();
  {
    unsigned ok = 0;
    for (unsigned spin = 0; spin < SPIN_CAP; ++spin) {
      if (__all(((bad4(ov, tag) | bad4(mv, tag)) & TAGM) == 0)) { ok = 1; break; }
      FENCE();
      ov = bld<AUX_SC1>(rws, off_o, 0);
      mv = bld<AUX_SC1>(rws, off_m, 0);
    }
    const float m = u2f((mv.x & 0xffff0000u) | (mv.y >> 16)), l = u2f((mv.z & 0xffff0000u) | (mv.w >> 16));
    const float M = hi3_max(m);
    const float om = __builtin_amdgcn_exp2f(m - M);
    const float Lt = hi3_sum(om * l);
    const float v = red4(f32x4{untag(ov[0]), untag(ov[1]), untag(ov[2]), untag(ov[3])} * om);
    if ((lane & 8) == 0) ((LAS float*)(L + L_OC))[w * 32 + 4 * (lane & 7) + (lane >> 4)] = v;
    if (lane == 0) { LAS float* omp = (LAS float*)(L + L_OM); omp[w] = M; omp[8 + w] = Lt; }
    if (lane == 0 && !ok) misc[1] = 1u;
  }
  unsigned off_po = WS_PO + (16 * ns + 2 * w + ((lane & 7) >> 2)) * 16384 + ks * 128 + ((lane & 3) * 8 + 4 * (lane >> 5)) * 4;
  off_po = (lane & 24) == 0 ? off_po : OOB;
  asm volatile("" : "+v"(off_po));
  BAR();
  {
    if (w == 4) {
      __builtin_amdgcn_s_setprio(3);
      LAS const f32x4* omp = (LAS const f32x4*)(L + L_OM);
      const f32x4 M0 = omp[0], M1 = omp[1], L0 = omp[2], L1 = omp[3];
      f32x4 oc[8];
#pragma unroll
      for (int ww = 0; ww < 8; ++ww) oc[ww] = *(LAS const f32x4*)(L + L_OC + (ww * 32 + 4 * (lane & 7)) * 4);
      SB();
      const float M = fmaxf(fmaxf(fmaxf(M0[0], M0[1]), fmaxf(M0[2], M0[3])), fmaxf(fmaxf(M1[0], M1[1]), fmaxf(M1[2], M1[3])));
      f32x4 ov = z4;
      float Lt = 0.f;
#pragma unroll
      for (int ww = 0; ww < 8; ++ww) {
        const float om = __builtin_amdgcn_exp2f((ww < 4 ? M0[ww & 3] : M1[ww & 3]) - M);
        Lt = fmaf(om, ww < 4 ? L0[ww & 3] : L1[ww & 3], Lt);
        ov += om * oc[ww];
      }
      const float fa = gs * __builtin_amdgcn_rcpf(Lt);
      if (lane < 8) *(LAS f32x4*)(L + L_XA + 16 * lane) = ov * fa;
    }
    BAR();
    const f32x4 xa4 = *(LAS const f32x4*)(L + L_XA + 16 * (lane >> 3));
    const unsigned dead = misc[1];
#pragma unroll
    for (int t = 0; t < 4; ++t)
#pragma unroll
      for (int i = 0; i < 4; ++i) a[i] += xa4[t] * wo_f[t][i];
    float bb[4];
    bb[0] = sw32_add(a[0].x, a[2].x); bb[1] = sw32_add(a[0].y, a[2].y); bb[2] = sw32_add(a[1].x, a[3].x); bb[3] = sw32_add(a[1].y, a[3].y);
#pragma unroll
    for (int i = 0; i < 4; ++i) { bb[i] = sw16_add(bb[i], bb[i]); bb[i] += dpp<ROR8>(bb[i]); }
    if (dead) return;
    const v4u o = {tagv(bb[0], tag), tagv(bb[1], tag), tagv(bb[2], tag), tagv(bb[3], tag)};
    bst_sc1(o, rws, off_po);
  }
  if (b < 32) {
    v4u pv[2];
    unsigned ok = 0;
    const unsigned off_f = WS_PO + b * 16384 + w * 2048 + lane * 16;
    for (unsigned spin = 0; spin < SPIN_CAP; ++spin) {
      FENCE();
#pragma unroll
      for (int k = 0; k < 2; ++k) pv[k] = bld<AUX_SC1>(rws, off_f, k * 1024);
      if (__all(((bad4(pv[0], tag) | bad4(pv[1], tag)) & TAGM) == 0)) { ok = 1; break; }
      __builtin_amdgcn_s_sleep(1);
    }
    const float v = red4(f32x4{untag(pv[0][0]) + untag(pv[1][0]), untag(pv[0][1]) + untag(pv[1][1]), untag(pv[0][2]) + untag(pv[1][2]), untag(pv[0][3]) + untag(pv[1][3])});
    if ((lane & 8) == 0) ((LAS float*)(L + L_FIN))[w * 32 + 4 * (lane & 7) + (lane >> 4)] = v;
    if (lane == 0 && !ok) misc[1] = 1u;
    BAR();
    if (w == 0 && lane < 8) {
      f32x4 fv[8];
#pragma unroll
      for (int ww = 0; ww < 8; ++ww) fv[ww] = *(LAS const f32x4*)(L + L_FIN + (ww * 32 + 4 * lane) * 4);
      const unsigned dead = misc[1];
      SB();
      const f32x4 s = ((fv[0] + fv[1]) + (fv[2] + fv[3])) + ((fv[4] + fv[5]) + (fv[6] + fv[7]));
      if (!dead) {
        const v2u o = {pkbf(s[0], s[1]), pkbf(s[2], s[3])};
        *(GAS v2u*)(p.out + 32 * b + 4 * lane) = o;
        if (b == 0 && lane == 0)
          __hip_atomic_store((GAS unsigned*)(p.ws + WS_EPOCH), epoch_v + 1u, __ATOMIC_RELAXED, __HIP_MEMORY_SCOPE_AGENT);
      }
    }
  }
}

}

extern "C" void kernel_launch(void* const* d_in, const int* in_sizes, int n_in, void* d_out, int out_size, void* d_ws, size_t ws_size,
                              hipStream_t stream) {
  Params p;
  p.x = (const unsigned short*)d_in[0];
  p.k = (const unsigned short*)d_in[1];
  p.v = (const unsigned short*)d_in[2];
  p.wq = (const unsigned short*)d_in[3];
  p.wo = (const unsigned short*)d_in[4];
  p.wg = (const unsigned short*)d_in[6];
  p.wu = (const unsigned short*)d_in[7];
  p.wd = (const unsigned short*)d_in[8];
  p.gw = (const unsigned short*)d_in[9];
  p.out = (unsigned short*)d_out;
  p.ws = (unsigned char*)d_ws;
  hipLaunchKernelGGL(fused_decode, dim3(NWG), dim3(NTHR), 0, stream, p);
}
```

```cpp
#include <hip/hip_runtime.h>
#include <hip/hip_bf16.h>
#include <stdint.h>

namespace {

#define GAS __attribute__((address_space(1)))
#define LAS __attribute__((address_space(3)))
typedef unsigned v4u __attribute__((ext_vector_type(4)));
typedef unsigned v2u __attribute__((ext_vector_type(2)));
typedef float f32x4 __attribute__((ext_vector_type(4)));
typedef float f32x2 __attribute__((ext_vector_type(2)));
typedef __bf16 bf16x8 __attribute__((ext_vector_type(8)));
typedef __bf16 bf16x2 __attribute__((ext_vector_type(2)));

constexpr int DM = 1024, SK = 65536, FF = 3584;
constexpr int NWG = 256, NTHR = 512;
constexpr int WI = 12;
constexpr int WK = 7;
constexpr int WP = 5;
constexpr int TWSTEP = 29;
constexpr int PACE_A = 315, PACE_B = 57;
constexpr int RD_E = 3, RD_O = 1, GST = 4;

constexpr int AUX_NT = 2, AUX_SC1 = 16, RSRC_W3 = 0x00020000;
constexpr unsigned TAGM = 0x3FFu;
constexpr unsigned SPIN_CAP = 1u << 15;
constexpr unsigned OOB = 0x7ffffff0u;

constexpr unsigned WS_EPOCH = 0;
constexpr unsigned WS_CL = 1024;
constexpr unsigned WS_QP = 4096;
constexpr unsigned WS_GU = WS_QP + 4 * 4 * 1024 * 4;
constexpr unsigned WS_OP = WS_GU + 2 * 4 * FF * 4;
constexpr unsigned WS_ML = WS_OP + NWG * 4096;
constexpr unsigned WS_PO = WS_ML + NWG * 128;
constexpr unsigned WS_END = WS_PO + 32 * 128 * 128;

constexpr int L_RED = 0;
constexpr int L_Q = L_RED + 3 * 8 * 64 * 4;
constexpr int L_ML = L_Q + 2048;
constexpr int L_OC = L_ML + 512;
constexpr int L_OM = L_OC + 1024;
constexpr int L_FIN = L_OM + 64;
constexpr int L_XA = L_FIN + 1024;
constexpr int L_H = L_XA + 128;
constexpr int L_MISC = L_H + 128;
constexpr int L_SLAB = L_MISC + 48;
constexpr int L_END = L_SLAB + 4 * 8 * 132 * 4;

struct Params {
  const unsigned short *x, *k, *v, *wq, *wo, *wg, *wu, *wd, *gw;
  unsigned short* out;
  unsigned char* ws;
};

__device__ __forceinline__ __amdgpu_buffer_rsrc_t mkrs(const void* p, unsigned bytes) {
  return __builtin_amdgcn_make_buffer_rsrc(const_cast<void*>(p), 0, (int)bytes, RSRC_W3);
}
template <int AUX> __device__ __forceinline__ v4u bld(__amdgpu_buffer_rsrc_t rs, unsigned voff, unsigned soff) {
  return __builtin_amdgcn_raw_buffer_load_b128(rs, (int)voff, (int)soff, AUX);
}
__device__ __forceinline__ void bst_sc1(v4u v, __amdgpu_buffer_rsrc_t rs, unsigned voff) {
  __builtin_amdgcn_raw_buffer_store_b128(v, rs, (int)voff, 0, AUX_SC1);
}
__device__ __forceinline__ float u2f(unsigned u) { return __builtin_bit_cast(float, u); }
__device__ __forceinline__ unsigned f2u(float f) { return __builtin_bit_cast(unsigned, f); }
__device__ __forceinline__ float bflo(unsigned u) { return u2f(u << 16); }
__device__ __forceinline__ float bfhi(unsigned u) { return u2f(u & 0xffff0000u); }
__device__ __forceinline__ unsigned tagv(float v, unsigned tag) { return ((f2u(v) + 0x200u) & ~TAGM) | tag; }
__device__ __forceinline__ float untag(unsigned u) { return u2f(u & ~TAGM); }
__device__ __forceinline__ unsigned bad4(v4u a, unsigned tag) { return ((a.x ^ tag) | (a.y ^ tag)) | ((a.z ^ tag) | (a.w ^ tag)); }
template <int CTRL> __device__ __forceinline__ float dpp(float x) {
  return u2f((unsigned)__builtin_amdgcn_mov_dpp((int)f2u(x), CTRL, 0xf, 0xf, true));
}
constexpr int ROR1 = 0x121, ROR2 = 0x122, ROR4 = 0x124, ROR8 = 0x128;
__device__ __forceinline__ float sw32_add(float a, float b) {
  auto s = __builtin_amdgcn_permlane32_swap(f2u(a), f2u(b), false, false);
  return u2f(s[0]) + u2f(s[1]);
}
__device__ __forceinline__ float sw16_add(float a, float b) {
  auto s = __builtin_amdgcn_permlane16_swap(f2u(a), f2u(b), false, false);
  return u2f(s[0]) + u2f(s[1]);
}
__device__ __forceinline__ float xrow_max(float x) {
  auto s = __builtin_amdgcn_permlane16_swap(f2u(x), f2u(x), false, false);
  x = fmaxf(u2f(s[0]), u2f(s[1]));
  auto t = __builtin_amdgcn_permlane32_swap(f2u(x), f2u(x), false, false);
  return fmaxf(u2f(t[0]), u2f(t[1]));
}
__device__ __forceinline__ float xrow_sum(float x) { x = sw16_add(x, x); return sw32_add(x, x); }
__device__ __forceinline__ float row_sum(float x) { x += dpp<ROR8>(x); x += dpp<ROR4>(x); x += dpp<ROR2>(x); x += dpp<ROR1>(x); return x; }
__device__ __forceinline__ float hi3_sum(float x) { x += dpp<ROR8>(x); return xrow_sum(x); }
__device__ __forceinline__ float hi3_max(float x) { x = fmaxf(x, dpp<ROR8>(x)); return xrow_max(x); }
__device__ __forceinline__ float red4(f32x4 v) {
  const float a = sw16_add(sw32_add(v[0], v[2]), sw32_add(v[1], v[3]));
  return a + dpp<ROR8>(a);
}
__device__ __forceinline__ f32x4 mfma32(v4u a, v4u b, f32x4 c) {
  return __builtin_amdgcn_mfma_f32_16x16x32_bf16(__builtin_bit_cast(bf16x8, a), __builtin_bit_cast(bf16x8, b), c, 0, 0, 0);
}
__device__ __forceinline__ unsigned pkbf(float lo, float hi) {
  bf16x2 t = {(__bf16)lo, (__bf16)hi};
  return __builtin_bit_cast(unsigned, t);
}
__device__ __forceinline__ void gemv4(const v4u (&w)[4], const float (&xs)[4], f32x2 (&a)[4]) {
#pragma unroll
  for (int t = 0; t < 4; ++t)
#pragma unroll
    for (int k = 0; k < 4; ++k) a[k] += xs[t] * f32x2{bflo(w[t][k]), bfhi(w[t][k])};
}
__device__ __forceinline__ float red8(const f32x2 (&a2)[4], int lane) {
  const float a[8] = {a2[0].x, a2[0].y, a2[1].x, a2[1].y, a2[2].x, a2[2].y, a2[3].x, a2[3].y};
  float b[4], c[2];
#pragma unroll
  for (int i = 0; i < 4; ++i) b[i] = sw32_add(a[i], a[i + 4]);
#pragma unroll
  for (int i = 0; i < 2; ++i) c[i] = sw16_add(b[i], b[i + 2]);
  const float c0 = c[0] + dpp<ROR8>(c[0]), c1 = c[1] + dpp<ROR8>(c[1]);
  return (lane & 8) ? c1 : c0;
}
#define SB() __builtin_amdgcn_sched_barrier(0)
#define BAR() do { asm volatile("s_waitcnt lgkmcnt(0)" ::: "memory"); __builtin_amdgcn_s_barrier(); asm volatile("" ::: "memory"); } while (0)
#define FENCE() asm volatile("" ::: "memory")

__global__ void __launch_bounds__(NTHR, 2) __attribute__((amdgpu_waves_per_eu(2, 2))) fused_decode(Params p) {
  __shared__ __attribute__((aligned(16))) unsigned char smem[L_END];
  LAS unsigned char* L = (LAS unsigned char*)smem;
  const int tid = threadIdx.x, lane = tid & 63, w = __builtin_amdgcn_readfirstlane(tid >> 6);
  const int b = blockIdx.x;
  const int G = (b >> 3) & 3, j = (b >> 5) * 8 + (b & 7);
  const int kq = j >> 4, c16 = j & 15;
  const int g = lane >> 4, c = lane & 15;
  const unsigned t_start = (unsigned)__builtin_amdgcn_s_memrealtime();
  __builtin_amdgcn_s_setprio(3);
  const __amdgpu_buffer_rsrc_t rws = mkrs(p.ws, WS_END);
  const unsigned epoch_raw = __builtin_amdgcn_raw_buffer_load_b32(rws, WS_EPOCH, 0, AUX_SC1);
  const int hw = w & 3;
  const bool has_gu = c16 < 14;
  const int ksl = j >> 1, ns = j & 1, ks = G * 32 + ksl;
  LAS float* red = (LAS float*)(L + L_RED);
  volatile LAS unsigned* misc = (volatile LAS unsigned*)(L + L_MISC);
  const int rcol = 8 * (lane & 7) + 4 * (lane >> 5) + 2 * ((lane >> 4) & 1) + ((lane >> 3) & 1);
  const f32x4 z4 = {0.f, 0.f, 0.f, 0.f};
  const __amdgpu_buffer_rsrc_t ro = mkrs(p.wo + (size_t)(1024 * G + 32 * ksl) * DM + 512 * ns + 64 * w, 32 * 2048 - 1024 * ns - 128 * w);
  const __amdgpu_buffer_rsrc_t rd = mkrs(p.wd + (size_t)(28 * ks) * DM + 512 * ns + 64 * w, 28 * 2048 - 1024 * ns - 128 * w);
  const unsigned vow = (lane >> 3) * 4 * 2048 + (lane & 7) * 16;
  v4u TW[8];
  f32x2 a[4], wo_f[4][4], wd_f[4][4];
  float gs;
  auto twprep_w = [&]() {
    asm volatile("" : "+v"(TW[0]), "+v"(TW[1]), "+v"(TW[2]), "+v"(TW[3]), "+v"(TW[4]), "+v"(TW[5]), "+v"(TW[6]), "+v"(TW[7]));
#pragma unroll
    for (int t = 0; t < 4; ++t)
#pragma unroll
      for (int k = 0; k < 4; ++k) { wo_f[t][k] = f32x2{bflo(TW[t][k]), bfhi(TW[t][k])}; wd_f[t][k] = f32x2{bflo(TW[4 + t][k]), bfhi(TW[4 + t][k])}; }
#pragma unroll
    for (int t = 0; t < 4; ++t) {
      asm volatile("" : "+v"(wo_f[t][0]), "+v"(wo_f[t][1]), "+v"(wo_f[t][2]), "+v"(wo_f[t][3]));
      asm volatile("" : "+v"(wd_f[t][0]), "+v"(wd_f[t][1]), "+v"(wd_f[t][2]), "+v"(wd_f[t][3]));
    }
  };
  auto twprep_d = [&]() {
    const f32x4 hv = *(LAS const f32x4*)(L + L_H + 16 * (lane >> 3));
    gs = u2f(misc[0]);
    const float gn = 1.f - gs;
#pragma unroll
    for (int i = 0; i < 4; ++i) a[i] = (hv[0] * gn) * wd_f[0][i];
#pragma unroll
    for (int t = 1; t < 4; ++t)
#pragma unroll
      for (int i = 0; i < 4; ++i) a[i] += (hv[t] * gn) * wd_f[t][i];
    asm volatile("" : "+v"(a[0]), "+v"(a[1]), "+v"(a[2]), "+v"(a[3]));
  };
  unsigned tag, epoch_v;
  if (w == 0) {
    if (lane < 8) { LAS unsigned* mlp = (LAS unsigned*)(L + L_ML) + lane;
#pragma unroll
      for (int ww = 0; ww < 4; ++ww) mlp[ww * 8] = 0xffffffffu; }
    if (lane < 9) ((LAS unsigned*)(L + L_MISC))[1 + lane] = 0u;
  }
  if (w >= 4) {
    const int rq0 = 256 * kq + 64 * hw;
    const v2u xq0 = *(const GAS v2u*)(p.x + rq0 + 4 * (lane >> 3)), xq1 = *(const GAS v2u*)(p.x + rq0 + 32 + 4 * (lane >> 3));
    v4u wa[8];
    {
      const __amdgpu_buffer_rsrc_t rq = mkrs(p.wq + (size_t)rq0 * 4096 + (16 * G + c16) * 64, 64 * 8192);
      const unsigned vo = (lane >> 3) * 4 * 8192 + (lane & 7) * 16;
#pragma unroll
      for (int t = 0; t < 8; ++t) wa[t] = bld<AUX_NT>(rq, vo, (t & 3) * 8192 + (t >> 2) * 32 * 8192);
    }
    SB();
    BAR();
    epoch_v = __builtin_amdgcn_readfirstlane(epoch_raw);
    const unsigned tag32 = epoch_v + 1u;
    tag = tag32 & TAGM;
    const bool xeven = (__builtin_amdgcn_s_getreg(20 | (3 << 11)) & 1) == 0;
    int tcA, tcB, tcC;
    bool upA = false, upC = true;
    if (has_gu) {
      tcA = c16; tcB = c16; tcC = xeven ? (c16 ^ 1) : -1;
      if ((c16 >> 1) < RD_E + RD_O) { upC = false; if ((c16 >> 1) >= GST) tcC = -1; }
    }
    else if (xeven) { upA = true; const int o_ = 1 - (c16 & 1); tcA = RD_E > 0 ? o_ : -1; tcB = RD_E > 1 ? 2 + o_ : -1; tcC = RD_E > 2 ? 4 + o_ : -1; }
    else { upA = true; const int o_ = 2 * RD_E + (c16 & 1); tcA = RD_O > 0 ? o_ : -1; tcB = RD_O > 1 ? 2 + o_ : -1; tcC = -1; }
    if (hw == 0 && lane < 3) {
      const int tc = lane == 0 ? tcA : lane == 1 ? tcB : tcC;
      if (tc >= 0 && tc != c16 && (lane == 2 || !has_gu))
        __hip_atomic_store((GAS unsigned*)(p.ws + WS_CL + ((lane == 2 && !upC) ? 1024 : 0) + ((G * 4 + kq) * 16 + tc) * 4), tag32, __ATOMIC_RELAXED, __HIP_MEMORY_SCOPE_AGENT);
    }
    const float xa[4] = {bflo(xq0.x), bfhi(xq0.x), bflo(xq0.y), bfhi(xq0.y)}, xb[4] = {bflo(xq1.x), bfhi(xq1.x), bflo(xq1.y), bfhi(xq1.y)};
    auto tile = [&](const v4u (&wt)[8], int mat, int cnt, unsigned dst) {
      f32x2 a[4];
#pragma unroll
      for (int i = 0; i < 4; ++i) a[i] = f32x2{0.f, 0.f};
      const v4u w0[4] = {wt[0], wt[1], wt[2], wt[3]}, w1[4] = {wt[4], wt[5], wt[6], wt[7]};
      gemv4(w0, xa, a);
      gemv4(w1, xb, a);
      red[(mat * 4 + hw) * 64 + rcol] = red8(a, lane);
      asm volatile("s_waitcnt lgkmcnt(0)" ::: "memory");
      unsigned old = 0;
      if (lane == 0) old = __hip_atomic_fetch_add((LAS unsigned*)(L + L_MISC) + cnt, 1u, __ATOMIC_RELAXED, __HIP_MEMORY_SCOPE_WORKGROUP);
      old = __builtin_amdgcn_readfirstlane(old);
      if (old == 3 && lane < 16) {
        FENCE();
        LAS const f32x4* r4 = (LAS const f32x4*)(L + L_RED) + mat * 64;
        const f32x4 sv = (r4[lane] + r4[16 + lane]) + (r4[32 + lane] + r4[48 + lane]);
        const v4u o = {tagv(sv[0], tag), tagv(sv[1], tag), tagv(sv[2], tag), tagv(sv[3], tag)};
        bst_sc1(o, rws, dst + lane * 16);
      }
    };
    tile(wa, 0, 2, WS_QP + ((G * 4 + kq) * 1024 + c16 * 64) * 4);
    __builtin_amdgcn_s_setprio(2);
    {
      v4u wb[8], wc[8], we[8];
      unsigned flu = 0;
      const unsigned vo = (lane >> 3) * 4 * 7168 + (lane & 7) * 16;
      if (!xeven && has_gu) {
        const unsigned fu = __builtin_amdgcn_raw_buffer_load_b32(rws, WS_CL + ((G * 4 + kq) * 16 + c16) * 4, 0, AUX_SC1);
        if (GST > 0 && (c16 >> 1) < GST) {
          const unsigned fg = __builtin_amdgcn_raw_buffer_load_b32(rws, WS_CL + 1024 + ((G * 4 + kq) * 16 + c16) * 4, 0, AUX_SC1);
          if (__builtin_amdgcn_readfirstlane(fg) == tag32) tcA = -1;
        }
        flu = fu;
      }
      const int cA = G * 14 + (tcA < 0 ? 0 : tcA), cC = G * 14 + (tcC < 0 ? 0 : tcC);
      const __amdgpu_buffer_rsrc_t rg = mkrs((upA ? p.wu : p.wg) + (size_t)rq0 * FF + cA * 64, tcA >= 0 ? 64 * 7168 - cA * 128 : 0);
#pragma unroll
      for (int t = 0; t < 8; ++t) wb[t] = bld<AUX_NT>(rg, vo + (t & 3) * 7168, (t >> 2) * 32 * 7168);
      SB();
      if (!xeven && has_gu && __builtin_amdgcn_readfirstlane(flu) == tag32) tcB = -1;
      const int cB = G * 14 + (tcB < 0 ? 0 : tcB);
      const __amdgpu_buffer_rsrc_t ru = mkrs(p.wu + (size_t)rq0 * FF + cB * 64, tcB >= 0 ? 64 * 7168 - cB * 128 : 0),
                                   rn = mkrs((upC ? p.wu : p.wg) + (size_t)rq0 * FF + cC * 64, tcC >= 0 ? 64 * 7168 - cC * 128 : 0);
#pragma unroll
      for (int t = 0; t < 8; ++t) wc[t] = bld<AUX_NT>(ru, vo + (t & 3) * 7168, (t >> 2) * 32 * 7168);
#pragma unroll
      for (int t = 0; t < 8; ++t) we[t] = bld<AUX_NT>(rn, vo + (t & 3) * 7168, (t >> 2) * 32 * 7168);
      SB();
      if (tcA >= 0) tile(wb, 1, 3, WS_GU + (((upA ? 1 : 0) * 4 + kq) * FF + cA * 64) * 4);
      if (tcB >= 0) tile(wc, 2, 8, WS_GU + ((1 * 4 + kq) * FF + cB * 64) * 4);
      if (tcC >= 0) tile(we, 3, 9, WS_GU + (((upC ? 1 : 0) * 4 + kq) * FF + cC * 64) * 4);
    }
    __builtin_amdgcn_s_setprio(3);
    unsigned ok = 0;
    v4u qv[4];
    for (unsigned spin = 0; spin < SPIN_CAP; ++spin) {
      FENCE();
#pragma unroll
      for (int n = 0; n < 4; ++n) qv[n] = bld<AUX_SC1>(rws, lane * 16 + hw * 1024, WS_QP + (G * 4 + n) * 4096);
      if (__all(((bad4(qv[0], tag) | bad4(qv[1], tag) | bad4(qv[2], tag) | bad4(qv[3], tag)) & TAGM) == 0)) { ok = 1; break; }
      __builtin_amdgcn_s_sleep(2);
    }
    float sq[4];
#pragma unroll
    for (int e = 0; e < 4; ++e) sq[e] = (untag(qv[0][e]) + untag(qv[1][e])) + (untag(qv[2][e]) + untag(qv[3][e]));
    const v2u o = {pkbf(sq[0], sq[1]), pkbf(sq[2], sq[3])};
    *(LAS v2u*)(L + L_Q + (256 * hw + 4 * lane) * 2) = o;
    if (lane == 0 && !ok) misc[1] = 1u;
    BAR();
    __builtin_amdgcn_s_setprio(1);
    if (hw == 0) {
      const int q7 = (lane & 7) < 7 ? (lane & 7) : 6;
      const unsigned off = WS_GU + (((lane >> 5) * 4 + ((lane >> 3) & 3)) * FF + 28 * ks + 4 * q7) * 4;
      v4u gv;
      unsigned okh = 0;
      for (unsigned spin = 0; spin < SPIN_CAP; ++spin) {
        FENCE();
        gv = bld<AUX_SC1>(rws, off, 0);
        if (__all((bad4(gv, tag) & TAGM) == 0)) { okh = 1; break; }
        __builtin_amdgcn_s_sleep(8);
      }
      f32x4 hv;
#pragma unroll
      for (int e = 0; e < 4; ++e) {
        float t = untag(gv[e]);
        t += dpp<ROR8>(t); t = sw16_add(t, t);
        auto sx = __builtin_amdgcn_permlane32_swap(f2u(t), f2u(t), false, false);
        const float gg = u2f(sx[0]), uu = u2f(sx[1]);
        hv[e] = (lane & 7) < 7 ? (gg > 0.f ? gg : 0.f * gg) * uu : 0.f;
      }
      if (lane < 8) *(LAS f32x4*)(L + L_H + 16 * lane) = hv;
      if (lane == 0 && !okh) misc[1] = 1u;
    } else if (hw == 1) {
      const v4u x0 = *(const GAS v4u*)(p.x + 16 * lane), x1 = *(const GAS v4u*)(p.x + 16 * lane + 8);
      const v4u g0 = *(const GAS v4u*)(p.gw + 16 * lane), g1 = *(const GAS v4u*)(p.gw + 16 * lane + 8);
      float sg = 0.f;
#pragma unroll
      for (int k = 0; k < 4; ++k) {
        sg = fmaf(bflo(x0[k]), bflo(g0[k]), sg); sg = fmaf(bfhi(x0[k]), bfhi(g0[k]), sg);
        sg = fmaf(bflo(x1[k]), bflo(g1[k]), sg); sg = fmaf(bfhi(x1[k]), bfhi(g1[k]), sg);
      }
      sg = xrow_sum(row_sum(sg));
      if (lane == 0) misc[0] = f2u(1.f / (1.f + __expf(-sg)));
    }
    while (misc[4 + hw] == 0u) __builtin_amdgcn_s_sleep(4);
    __builtin_amdgcn_s_setprio(2);
#pragma unroll
    for (int q_ = 0; q_ < 4; ++q_) TW[q_] = bld<AUX_NT>(ro, vow + q_ * 2048, 0);
#pragma unroll
    for (int q_ = 0; q_ < 4; ++q_) TW[4 + q_] = bld<AUX_NT>(rd, vow + q_ * 2048, 0);
    SB();
    BAR();
  } else {
    const size_t hb = (size_t)G * SK * 128 + (size_t)(8 * j + 2 * w) * 2048;
    const unsigned hn = (SK * 256) - (8 * j + 2 * w) * 4096;
    const __amdgpu_buffer_rsrc_t rk = mkrs(p.k + hb, hn), rv = mkrs(p.v + hb, hn);
    const unsigned vok = (lane & 7) * 256 + (((lane >> 3) & 1) * 4 + g) * 16;
    const unsigned vov = g * 1024 + c * 16;
    unsigned proff[8];
#pragma unroll
    for (int pi = 0; pi < 8; ++pi) proff[pi] = ((pi + G) & 7) * (512u * 4096u);
    constexpr int NU = 34;
    v4u U[NU][4];
#define ISSUE(n) do { _Pragma("unroll") for (int q_ = 0; q_ < 4; ++q_) { \
    if ((n) < 16) U[n][q_] = bld<AUX_NT>(rk, vok + (q_ >> 1) * 2048 + (q_ & 1) * 128, proff[(n) >> 1] + ((n) & 1) * 4096u); \
    else if ((n) < 32) U[n][q_] = bld<AUX_NT>(rv, vov + q_ * 256, proff[((n) - 16) >> 1] + ((n) & 1) * 4096u); \
    else if ((n) == 32) U[n][q_] = bld<AUX_NT>(ro, vow + q_ * 2048, 0); \
    else U[n][q_] = bld<AUX_NT>(rd, vow + q_ * 2048, 0); } SB(); } while (0)
#define THROTTLE(u) do { if ((int)((unsigned)__builtin_amdgcn_s_memrealtime() - t_start) < PACE_A + PACE_B * (u)) asm volatile("s_waitcnt vmcnt(3)" ::: "memory"); } while (0)
    __builtin_amdgcn_s_setprio(1);
    ISSUE(0);
    BAR();
#pragma unroll
    for (int n = 1; n < WI; ++n) ISSUE(n);
    epoch_v = __builtin_amdgcn_readfirstlane(epoch_raw);
    tag = (epoch_v + 1u) & TAGM;
    BAR();
    v4u QA[2], QB[2];
    {
      const int hi = (lane >> 3) & 1;
#pragma unroll
      for (int u = 0; u < 2; ++u) {
        QA[u] = *(LAS const v4u*)(L + L_Q + (lane & 7) * 256 + (8 * u + 4 * hi + g) * 16);
        QB[u] = *(LAS const v4u*)(L + L_Q + (lane & 7) * 256 + (8 * u + 4 * (1 - hi) + g) * 16);
      }
    }
    const bool validA = (lane >> 5) == ((lane >> 3) & 1);
    float S[8][4];
    f32x4 P1, P2;
#pragma unroll
    for (int n = 0; n < 16; ++n) {
      if (n + WK >= WI && n + WK < 16 + WP) { THROTTLE(n + WK); ISSUE(n + WK); }
      if ((n & 1) == 0) {
        P1 = mfma32(U[n][0], QA[0], z4); P1 = mfma32(U[n][1], QA[1], P1);
        P2 = mfma32(U[n][2], QA[0], z4); P2 = mfma32(U[n][3], QA[1], P2);
      } else {
        f32x4 R1 = mfma32(U[n][0], QB[0], z4); R1 = mfma32(U[n][1], QB[1], R1);
        f32x4 R2 = mfma32(U[n][2], QB[0], z4); R2 = mfma32(U[n][3], QB[1], R2);
#pragma unroll
        for (int i = 0; i < 4; ++i) {
          const float m1 = validA ? P1[i] : R1[i], m2 = validA ? P2[i] : R2[i];
          auto sx = __builtin_amdgcn_permlane32_swap(f2u(m1), f2u(m2), false, false);
          S[n >> 1][i] = u2f(sx[0]) + dpp<ROR8>(u2f(sx[1]));
        }
      }
      SB();
    }
    constexpr float C2 = 0.08838834764831845f * 1.4426950408889634f;
    float mx = S[0][0];
#pragma unroll
    for (int qd = 0; qd < 8; ++qd)
#pragma unroll
      for (int i = 0; i < 4; ++i) mx = fmaxf(mx, S[qd][i]);
    mx = hi3_max(mx) * C2;
    if (lane < 8) ((LAS float*)(L + L_ML))[w * 8 + lane] = mx;
    float ls = 0.f;
    f32x4 acc[8];
#pragma unroll
    for (int i = 0; i < 8; ++i) acc[i] = z4;
    float Mcu = 0.f, omw = 0.f;
#pragma unroll
    for (int n = 16; n < 32; ++n) {
      if (n + WP < NU) { if (n + WP < 32) THROTTLE(n + WP); ISSUE(n + WP); }
      if (n == TWSTEP && lane == 0) misc[4 + w] = 1u;
      if ((n & 1) == 0) {
        const int qd = (n - 16) >> 1;
#pragma unroll
        for (int i = 0; i < 4; ++i) { S[qd][i] = __builtin_amdgcn_exp2f(fmaf(S[qd][i], C2, -mx)); ls += S[qd][i]; }
        if (n == 30) ls = hi3_sum(ls);
        SB();
      }
      if (n == 30) {
        LAS const volatile unsigned* mlp = (LAS const volatile unsigned*)(L + L_ML) + (lane & 7);
        unsigned mm[4];
        for (;;) {
          bool all = true;
#pragma unroll
          for (int ww = 0; ww < 4; ++ww) { mm[ww] = mlp[ww * 8]; all = all && mm[ww] != 0xffffffffu; }
          if (__all(all)) break;
          __builtin_amdgcn_s_sleep(1);
        }
        Mcu = fmaxf(fmaxf(u2f(mm[0]), u2f(mm[1])), fmaxf(u2f(mm[2]), u2f(mm[3])));
        omw = __builtin_amdgcn_exp2f(mx - Mcu);
        if (lane < 8) { ((LAS float*)(L + L_ML))[32 + w * 8 + lane] = ls * omw; if (w == 0) ((LAS float*)(L + L_ML))[64 + lane] = Mcu; }
        SB();
      }
      if (n & 1) {
        const int qd = (n - 16) >> 1;
        const unsigned a0 = pkbf(S[qd][0], S[qd][1]), a1 = pkbf(S[qd][2], S[qd][3]);
        const v4u PPq = {a0, a1, f2u(dpp<ROR8>(u2f(a0))), f2u(dpp<ROR8>(u2f(a1)))};
#pragma unroll
        for (int wd = 0; wd < 4; ++wd)
#pragma unroll
          for (int hf = 0; hf < 2; ++hf) {
            const unsigned sel = hf ? 0x07060302u : 0x05040100u;
            const v4u A = {__builtin_amdgcn_perm(U[n - 1][1][wd], U[n - 1][0][wd], sel), __builtin_amdgcn_perm(U[n - 1][3][wd], U[n - 1][2][wd], sel),
                           __builtin_amdgcn_perm(U[n][1][wd], U[n][0][wd], sel), __builtin_amdgcn_perm(U[n][3][wd], U[n][2][wd], sel)};
            acc[2 * wd + hf] = mfma32(A, PPq, acc[2 * wd + hf]);
          }
      }
      SB();
    }
#pragma unroll
    for (int n = 32 + WP; n < NU; ++n) ISSUE(n);
#undef ISSUE
#undef THROTTLE
    if (c < 8) {
      LAS f32x4* sl = (LAS f32x4*)(L + L_SLAB + ((w * 8 + c) * 132 + 32 * g) * 4);
#pragma unroll
      for (int rr = 0; rr < 4; ++rr) {
        sl[2 * rr] = f32x4{acc[0][rr], acc[1][rr], acc[2][rr], acc[3][rr]} * omw;
        sl[2 * rr + 1] = f32x4{acc[4][rr], acc[5][rr], acc[6][rr], acc[7][rr]} * omw;
      }
    }
#pragma unroll
    for (int q_ = 0; q_ < 4; ++q_) { TW[q_] = U[32][q_]; TW[4 + q_] = U[33][q_]; }
    BAR();
  }
  if (w >= 4) {
    const int t2 = tid - 256, h = t2 >> 5, d4 = t2 & 31;
    f32x4 sv[4];
#pragma unroll
    for (int ww = 0; ww < 4; ++ww) sv[ww] = *(LAS const f32x4*)(L + L_SLAB + ((ww * 8 + h) * 132 + 4 * d4) * 4);
    LAS const float* mlp = (LAS const float*)(L + L_ML);
    const float l0 = mlp[32 + h], l1 = mlp[40 + h], l2 = mlp[48 + h], l3 = mlp[56 + h], Mh = mlp[64 + h];
    SB();
    const f32x4 o = (sv[0] + sv[1]) + (sv[2] + sv[3]);
    const v4u ov = {tagv(o[0], tag), tagv(o[1], tag), tagv(o[2], tag), tagv(o[3], tag)};
    bst_sc1(ov, rws, WS_OP + ((G * 32 + h * 4 + (d4 >> 3)) * 64 + j) * 128 + (d4 & 7) * 16);
    {
      const unsigned mb = f2u(Mh), lb = f2u((l0 + l1) + (l2 + l3));
      const v4u mv = {(mb & 0xffff0000u) | tag, (mb << 16) | tag, (lb & 0xffff0000u) | tag, (lb << 16) | tag};
      bst_sc1(mv, rws, d4 == 0 ? WS_ML + ((G * 8 + h) * 64 + j) * 16 : OOB);
    }
    SB();
    twprep_w();
    twprep_d();
  } else {
    twprep_w();
    twprep_d();
  }
  const int hh = ksl >> 2;
  const unsigned off_o = WS_OP + (G * 32 + ksl) * 8192 + w * 1024 + lane * 16;
  const unsigned off_m = WS_ML + ((G * 8 + hh) * 64 + 8 * w + (lane >> 3)) * 16;
  v4u ov, mv;
  __builtin_amdgcn_s_sleep(10);
  FENCE();
  ov = bld<AUX_SC1>(rws, off_o, 0); mv = bld<AUX_SC1>(rws, off_m, 0);
  SB();
  {
    unsigned ok = 0;
    for (unsigned spin = 0; spin < SPIN_CAP; ++spin) {
      if (__all(((bad4(ov, tag) | bad4(mv, tag)) & TAGM) == 0)) { ok = 1; break; }
      FENCE();
      ov = bld<AUX_SC1>(rws, off_o, 0);
      mv = bld<AUX_SC1>(rws, off_m, 0);
    }
    const float m = u2f((mv.x & 0xffff0000u) | (mv.y >> 16)), l = u2f((mv.z & 0xffff0000u) | (mv.w >> 16));
    const float M = hi3_max(m);
    const float om = __builtin_amdgcn_exp2f(m - M);
    const float Lt = hi3_sum(om * l);
    const float v = red4(f32x4{untag(ov[0]), untag(ov[1]), untag(ov[2]), untag(ov[3])} * om);
    if ((lane & 8) == 0) ((LAS float*)(L + L_OC))[w * 32 + 4 * (lane & 7) + (lane >> 4)] = v;
    if (lane == 0) { LAS float* omp = (LAS float*)(L + L_OM); omp[w] = M; omp[8 + w] = Lt; }
    if (lane == 0 && !ok) misc[1] = 1u;
  }
  unsigned off_po = WS_PO + (16 * ns + 2 * w + ((lane & 7) >> 2)) * 16384 + ks * 128 + ((lane & 3) * 8 + 4 * (lane >> 5)) * 4;
  off_po = (lane & 24) == 0 ? off_po : OOB;
  asm volatile("" : "+v"(off_po));
  BAR();
  {
    if (w == 4) {
      __builtin_amdgcn_s_setprio(3);
      LAS const f32x4* omp = (LAS const f32x4*)(L + L_OM);
      const f32x4 M0 = omp[0], M1 = omp[1], L0 = omp[2], L1 = omp[3];
      f32x4 oc[8];
#pragma unroll
      for (int ww = 0; ww < 8; ++ww) oc[ww] = *(LAS const f32x4*)(L + L_OC + (ww * 32 + 4 * (lane & 7)) * 4);
      SB();
      const float M = fmaxf(fmaxf(fmaxf(M0[0], M0[1]), fmaxf(M0[2], M0[3])), fmaxf(fmaxf(M1[0], M1[1]), fmaxf(M1[2], M1[3])));
      f32x4 ov = z4;
      float Lt = 0.f;
#pragma unroll
      for (int ww = 0; ww < 8; ++ww) {
        const float om = __builtin_amdgcn_exp2f((ww < 4 ? M0[ww & 3] : M1[ww & 3]) - M);
        Lt = fmaf(om, ww < 4 ? L0[ww & 3] : L1[ww & 3], Lt);
        ov += om * oc[ww];
      }
      const float fa = gs * __builtin_amdgcn_rcpf(Lt);
      if (lane < 8) *(LAS f32x4*)(L + L_XA + 16 * lane) = ov * fa;
    }
    BAR();
    const f32x4 xa4 = *(LAS const f32x4*)(L + L_XA + 16 * (lane >> 3));
    const unsigned dead = misc[1];
#pragma unroll
    for (int t = 0; t < 4; ++t)
#pragma unroll
      for (int i = 0; i < 4; ++i) a[i] += xa4[t] * wo_f[t][i];
    float bb[4];
    bb[0] = sw32_add(a[0].x, a[2].x); bb[1] = sw32_add(a[0].y, a[2].y); bb[2] = sw32_add(a[1].x, a[3].x); bb[3] = sw32_add(a[1].y, a[3].y);
#pragma unroll
    for (int i = 0; i < 4; ++i) { bb[i] = sw16_add(bb[i], bb[i]); bb[i] += dpp<ROR8>(bb[i]); }
    if (dead) return;
    const v4u o = {tagv(bb[0], tag), tagv(bb[1], tag), tagv(bb[2], tag), tagv(bb[3], tag)};
    bst_sc1(o, rws, off_po);
  }
  if (b < 32) {
    v4u pv[2];
    unsigned ok = 0;
    const unsigned off_f = WS_PO + b * 16384 + w * 2048 + lane * 16;
    for (unsigned spin = 0; spin < SPIN_CAP; ++spin) {
      FENCE();
#pragma unroll
      for (int k = 0; k < 2; ++k) pv[k] = bld<AUX_SC1>(rws, off_f, k * 1024);
      if (__all(((bad4(pv[0], tag) | bad4(pv[1], tag)) & TAGM) == 0)) { ok = 1; break; }
      __builtin_amdgcn_s_sleep(1);
    }
    const float v = red4(f32x4{untag(pv[0][0]) + untag(pv[1][0]), untag(pv[0][1]) + untag(pv[1][1]), untag(pv[0][2]) + untag(pv[1][2]), untag(pv[0][3]) + untag(pv[1][3])});
    if ((lane & 8) == 0) ((LAS float*)(L + L_FIN))[w * 32 + 4 * (lane & 7) + (lane >> 4)] = v;
    if (lane == 0 && !ok) misc[1] = 1u;
    BAR();
    if (w == 0 && lane < 8) {
      f32x4 fv[8];
#pragma unroll
      for (int ww = 0; ww < 8; ++ww) fv[ww] = *(LAS const f32x4*)(L + L_FIN + (ww * 32 + 4 * lane) * 4);
      const unsigned dead = misc[1];
      SB();
      const f32x4 s = ((fv[0] + fv[1]) + (fv[2] + fv[3])) + ((fv[4] + fv[5]) + (fv[6] + fv[7]));
      if (!dead) {
        const v2u o = {pkbf(s[0], s[1]), pkbf(s[2], s[3])};
        *(GAS v2u*)(p.out + 32 * b + 4 * lane) = o;
        if (b == 0 && lane == 0)
          __hip_atomic_store((GAS unsigned*)(p.ws + WS_EPOCH), epoch_v + 1u, __ATOMIC_RELAXED, __HIP_MEMORY_SCOPE_AGENT);
      }
    }
  }
}

}

extern "C" void kernel_launch(void* const* d_in, const int* in_sizes, int n_in, void* d_out, int out_size, void* d_ws, size_t ws_size,
                              hipStream_t stream) {
  Params p;
  p.x = (const unsigned short*)d_in[0];
  p.k = (const unsigned short*)d_in[1];
  p.v = (const unsigned short*)d_in[2];
  p.wq = (const unsigned short*)d_in[3];
  p.wo = (const unsigned short*)d_in[4];
  p.wg = (const unsigned short*)d_in[6];
  p.wu = (const unsigned short*)d_in[7];
  p.wd = (const unsigned short*)d_in[8];
  p.gw = (const unsigned short*)d_in[9];
  p.out = (unsigned short*)d_out;
  p.ws = (unsigned char*)d_ws;
  hipLaunchKernelGGL(fused_decode, dim3(NWG), dim3(NTHR), 0, stream, p);
}
```
